```python
import torch

def _softmax(x, axis=-1):
    return torch.softmax(x, dim=axis)
def _gelu(x):
    return torch.nn.functional.gelu(x, approximate="tanh")
def _silu(x):
    return torch.nn.functional.silu(x)
def _sigmoid(x):
    return torch.sigmoid(x)
def _layernorm(x, axis=-1, eps=1e-5):
    m = x.mean(dim=axis, keepdim=True)
    v = x.var(dim=axis, keepdim=True, unbiased=False)
    return (x - m) / torch.sqrt(v + eps)
def _scatter_add(tgt, idx, src, axis=0):
    return tgt.clone().index_add_(axis, idx, src)
def _segment_sum(src, seg, n):
    out = torch.zeros((n,) + src.shape[1:], dtype=src.dtype)
    return out.index_add_(0, seg, src)
def _pad(x, pw):
    flat = tuple(v for pair in reversed(pw) for v in pair)
    return torch.nn.functional.pad(x, flat)

kernel_name = 'standalone__paged_block_table_kv_pagedattention__decode'

def setup_inputs():

    _w = (lambda a: (lambda t: t.to(torch.bfloat16) if t.is_floating_point() else t)(torch.as_tensor(a)))
    B, Hq, Hkv, Sq, Skv, D = 1, 16, 8, 1, 65536, 256
    bs = 16
    nbps = max(1, Skv // bs)
    num_phys = B * nbps + 4
    bt = torch.randperm(num_phys)[: B * nbps].reshape(B, nbps).to(torch.int64)
    cl = (nbps * bs - (torch.arange(1, B + 1, dtype=torch.int64) * 2654435761) % max(1, nbps * bs // 2)).to(torch.int64)
    return {
        "q": _w((lambda *s: torch.randn(*s, dtype=torch.bfloat16) * 0.8)(B, Hq, Sq, D)),
        "k_cache": _w((lambda *s: torch.randn(*s, dtype=torch.bfloat16) * 0.8)(num_phys, bs, Hkv, D)),
        "v_cache": _w((lambda *s: torch.randn(*s, dtype=torch.bfloat16) * 0.05)(num_phys, bs, Hkv, D)),
        "block_table": _w(bt),
        "context_lens": _w(cl),
    }

def reference(q, k_cache, v_cache, block_table, context_lens):
    B, Hq, Sq, D = q.shape
    bs = k_cache.shape[1]
    Hkv = k_cache.shape[2]
    nbps = block_table.shape[1]
    Skv = nbps * bs
    bt = block_table.reshape(-1)
    k = (lambda x, *d: x.permute(*d))(k_cache[bt].reshape(B, Skv, Hkv, D), 0, 2, 1, 3)
    v = (lambda x, *d: x.permute(*d))(v_cache[bt].reshape(B, Skv, Hkv, D), 0, 2, 1, 3)
    g = Hq // Hkv
    qg = q.reshape(B, Hkv, g, Sq, D)
    scores = torch.einsum("bhgqd,bhkd->bhgqk", qg, k) * (D ** -0.5)
    pos = torch.arange(Skv)
    msk = pos[None, :] < context_lens[:, None]
    scores = torch.where(msk[:, None, None, None, :], scores, -1e9)
    p = _softmax(scores, axis=-1)
    out = torch.einsum("bhgqk,bhkd->bhgqd", p, v)
    return out.reshape(B, Hq, Sq, D)
```

```cpp
#include <hip/hip_runtime.h>
#include <hip/hip_bf16.h>
#include <stdint.h>

namespace {

constexpr int D = 256, PAGE = 16, HQ = 16, HKV = 8;
constexpr int NSPLIT = 32, WAVES = 8, NTHR = WAVES * 64;
constexpr int PAGE_BYTES = PAGE * HKV * D * 2;
constexpr int TOK_BYTES = HKV * D * 2;
constexpr int ROW_BYTES = D * 2;
constexpr int PSTRIDE = NSPLIT * WAVES;
constexpr float C2 = 0.0625f * 1.4426950408889634f;

using bf16x2 = __attribute__((ext_vector_type(2))) __bf16;
using u32x4 = __attribute__((ext_vector_type(4))) unsigned;
using f32x2 = __attribute__((ext_vector_type(2))) float;
using f32x4 = __attribute__((ext_vector_type(4))) float;

__device__ __forceinline__ float dot2(unsigned k, unsigned q, float acc) {
  return __builtin_amdgcn_fdot2_f32_bf16(__builtin_bit_cast(bf16x2, k), __builtin_bit_cast(bf16x2, q), acc, false);
}
template <int CTRL> __device__ __forceinline__ float dpp(float x) {
  return __builtin_bit_cast(float, __builtin_amdgcn_mov_dpp(__builtin_bit_cast(int, x), CTRL, 0xf, 0xf, true));
}
constexpr int XOR1 = 0xB1, XOR2 = 0x4E, XOR7 = 0x141, ROR8 = 0x128, ROR4 = 0x124, ROR2 = 0x122, BCAST = 0x150;

struct Pair { float a, b; };
__device__ __forceinline__ Pair swap16(float a, float b) {
  auto s = __builtin_amdgcn_permlane16_swap(__float_as_uint(a), __float_as_uint(b), false, false);
  return {__uint_as_float(s[0]), __uint_as_float(s[1])};
}
__device__ __forceinline__ Pair swap32(float a, float b) {
  auto s = __builtin_amdgcn_permlane32_swap(__float_as_uint(a), __float_as_uint(b), false, false);
  return {__uint_as_float(s[0]), __uint_as_float(s[1])};
}

constexpr int AUX_NT = 2;

__device__ __forceinline__ void issue8(u32x4 (&buf)[8], const void* base, unsigned bytes, unsigned voff, unsigned soff) {
  const __amdgpu_buffer_rsrc_t r = __builtin_amdgcn_make_buffer_rsrc(const_cast<void*>(base), 0, bytes, 0x00020000);
  __builtin_amdgcn_sched_barrier(0);
#pragma unroll
  for (int i = 0; i < 8; ++i) buf[i] = __builtin_amdgcn_raw_buffer_load_b128(r, voff, soff + i * 2 * TOK_BYTES, AUX_NT);
  asm volatile("" ::: "memory");
  __builtin_amdgcn_sched_barrier(0);
}

struct State { float m, l; f32x2 acc[2][4]; };

__global__ void __launch_bounds__(NTHR) attn_split(const uint16_t* q, const uint16_t* kc,
                                                  const uint16_t* vc, const int* __restrict__ bt,
                                                  const int* __restrict__ lens, float* __restrict__ ws,
                                                  int nbps, unsigned cache_bytes) {
  __shared__ __attribute__((aligned(16))) float lds[WAVES * 2 * D + WAVES * 4];
  const int s = blockIdx.x & (NSPLIT - 1), h = blockIdx.x / NSPLIT;
  const int tid = threadIdx.x, lane = tid & 63, wave = __builtin_amdgcn_readfirstlane(tid >> 6);
  const int hw = lane >> 5, c = lane & 31;
  const int pg0 = s + NSPLIT * wave;
  const int btv = bt[min(pg0 + PSTRIDE * lane, nbps - 1)];
  u32x4 qv[2];
  qv[0] = *reinterpret_cast<const u32x4*>(q + (2 * h) * D + 8 * c);
  qv[1] = *reinterpret_cast<const u32x4*>(q + (2 * h + 1) * D + 8 * c);
  int len = lens[0];
  const int skv = nbps * PAGE;
  const bool uniform = len <= 0;
  len = (uniform || len > skv) ? skv : len;
  const int npages = (len + PAGE - 1) / PAGE;
  const int n = npages > pg0 ? (npages - pg0 + PSTRIDE - 1) / PSTRIDE : 0;
  const unsigned voff = hw * TOK_BYTES + c * 16;
  const unsigned hoff = h * ROW_BYTES;
  const int tok = 2 * ((lane >> 1) & 7) + hw;

  u32x4 KA[8], VA[8], KB[8], VB[8];
  auto soff_of = [&](int i) { return (unsigned)__builtin_amdgcn_readlane(btv, i & 63) * (unsigned)PAGE_BYTES + hoff; };
  auto bytes_of = [&](int i) { return (unsigned)i < (unsigned)n ? cache_bytes : 0u; };
  const int i0 = -(n & 1);
  issue8(KA, kc, bytes_of(i0), voff, soff_of(i0));
  issue8(VA, vc, bytes_of(i0), voff, soff_of(i0));
  issue8(KB, kc, bytes_of(i0 + 1), voff, soff_of(i0 + 1));
  issue8(VB, vc, bytes_of(i0 + 1), voff, soff_of(i0 + 1));

  float m = -INFINITY, l = 0.f;
  f32x2 acc[2][4];
#pragma unroll
  for (int g = 0; g < 2; ++g)
#pragma unroll
    for (int k = 0; k < 4; ++k) acc[g][k] = f32x2{0.f, 0.f};

  auto step = [&](u32x4 (&K)[8], u32x4 (&V)[8], int i) {
    float y[8];
#pragma unroll
    for (int t = 0; t < 8; ++t) {
      float x0 = 0.f, x1 = 0.f;
#pragma unroll
      for (int k = 0; k < 4; ++k) { x0 = dot2(K[t][k], qv[0][k], x0); x1 = dot2(K[t][k], qv[1][k], x1); }
      const Pair p = swap16(x0, x1);
      y[t] = p.a + p.b;
    }
    const unsigned so = soff_of(i + 2), by = bytes_of(i + 2);
    issue8(K, kc, by, voff, so);
#pragma unroll
    for (int j = 0; j < 4; ++j) { const float t1 = y[j] + dpp<ROR8>(y[j]), t2 = y[j + 4] + dpp<ROR8>(y[j + 4]); y[j] = (lane & 8) ? t2 : t1; }
#pragma unroll
    for (int j = 0; j < 2; ++j) { const float t1 = y[j] + dpp<XOR7>(y[j]), t2 = y[j + 2] + dpp<XOR7>(y[j + 2]); y[j] = (lane & 4) ? t2 : t1; }
    { const float t1 = y[0] + dpp<XOR2>(y[0]), t2 = y[1] + dpp<XOR2>(y[1]); y[0] = (lane & 2) ? t2 : t1; }
    float sc = y[0] + dpp<XOR1>(y[0]);
    const int rem = i < 0 ? 0 : len - (pg0 + PSTRIDE * i) * PAGE;
    sc = uniform ? 0.f : sc * C2;
    sc = tok < rem ? sc : -INFINITY;
    float pm = fmaxf(sc, dpp<ROR8>(sc)); pm = fmaxf(pm, dpp<ROR4>(pm)); pm = fmaxf(pm, dpp<ROR2>(pm));
    const float mn = fmaxf(m, pm), msub = (mn == -INFINITY) ? 0.f : mn;
    const float alpha = __builtin_amdgcn_exp2f(m - msub), p = __builtin_amdgcn_exp2f(sc - msub);
    l = fmaf(l, alpha, p); m = mn;
    const Pair al = swap16(alpha, alpha), pp = swap16(p, p);
#pragma unroll
    for (int k = 0; k < 4; ++k) { acc[0][k] *= al.a; acc[1][k] *= al.b; }
#define PV(T) { const float p0 = dpp<BCAST + 2 * T>(pp.a), p1 = dpp<BCAST + 2 * T>(pp.b); \
      _Pragma("unroll") for (int k = 0; k < 4; ++k) { \
        const f32x2 vf = {__builtin_bit_cast(float, V[T][k] << 16), __builtin_bit_cast(float, V[T][k] & 0xffff0000u)}; \
        acc[0][k] += f32x2{p0, p0} * vf; acc[1][k] += f32x2{p1, p1} * vf; } }
    PV(0) PV(1) PV(2) PV(3) PV(4) PV(5) PV(6) PV(7)
#undef PV
    issue8(V, vc, by, voff, so);
  };

  for (int i = i0; i < n; i += 2) {
    step(KA, VA, i);
    step(KB, VB, i + 1);
  }

  l += dpp<ROR8>(l); l += dpp<ROR4>(l); l += dpp<ROR2>(l);
  { const Pair mm = swap32(m, m); const float M = fmaxf(mm.a, mm.b), Ms = (M == -INFINITY) ? 0.f : M;
    const float w = __builtin_amdgcn_exp2f(m - Ms);
    const Pair ll = swap32(l * w, l * w); l = ll.a + ll.b; m = M;
    const Pair ww = swap16(w, w);
    float o[8];
#pragma unroll
    for (int k = 0; k < 4; ++k) {
      const Pair x = swap32(acc[0][k].x * ww.a, acc[1][k].x * ww.b), y2 = swap32(acc[0][k].y * ww.a, acc[1][k].y * ww.b);
      o[2 * k] = x.a + x.b; o[2 * k + 1] = y2.a + y2.b; }
    float* rec = lds + (wave * 2 + hw) * D + 8 * c;
    *reinterpret_cast<f32x4*>(rec) = f32x4{o[0], o[1], o[2], o[3]};
    *reinterpret_cast<f32x4*>(rec + 4) = f32x4{o[4], o[5], o[6], o[7]};
    if ((lane & 47) == 0) { float* ml = lds + WAVES * 2 * D + wave * 4 + 2 * (lane >> 4); ml[0] = m; ml[1] = l; }
  }
  __syncthreads();
  { const int g = tid >> 8, d = tid & 255;
    const float* ml = lds + WAVES * 2 * D + 2 * g;
    float M = -INFINITY;
#pragma unroll
    for (int w = 0; w < WAVES; ++w) M = fmaxf(M, ml[4 * w]);
    const float Ms = (M == -INFINITY) ? 0.f : M;
    float L = 0.f, o = 0.f;
#pragma unroll
    for (int w = 0; w < WAVES; ++w) { const float wt = __builtin_amdgcn_exp2f(ml[4 * w] - Ms);
      L = fmaf(wt, ml[4 * w + 1], L); o = fmaf(wt, lds[(w * 2 + g) * D + d], o); }
    const int row = 2 * h + g;
    ws[(row * NSPLIT + s) * D + d] = o;
    if (d == 0) { float* pml = ws + HQ * NSPLIT * D + (row * NSPLIT + s) * 2; pml[0] = M; pml[1] = L; }
  }
}

__global__ void __launch_bounds__(D) attn_combine(const float* __restrict__ ws, __hip_bfloat16* __restrict__ out) {
  const int row = blockIdx.x, d = threadIdx.x;
  const f32x2* pml = reinterpret_cast<const f32x2*>(ws + HQ * NSPLIT * D) + row * NSPLIT;
  const float* po = ws + row * NSPLIT * D + d;
  float a[NSPLIT]; f32x2 ml[NSPLIT];
#pragma unroll
  for (int s = 0; s < NSPLIT; ++s) { a[s] = po[s * D]; ml[s] = pml[s]; }
  float M = -INFINITY;
#pragma unroll
  for (int s = 0; s < NSPLIT; ++s) M = fmaxf(M, ml[s].x);
  float L = 0.f, o = 0.f;
#pragma unroll
  for (int s = 0; s < NSPLIT; ++s) { const float w = __builtin_amdgcn_exp2f(ml[s].x - M); L = fmaf(w, ml[s].y, L); o = fmaf(w, a[s], o); }
  out[row * D + d] = __float2bfloat16(o / L);
}

}

extern "C" void kernel_launch(void* const* d_in, const int* in_sizes, int n_in, void* d_out, int out_size,
                              void* d_ws, size_t ws_size, hipStream_t stream) {
  const int nbps = in_sizes[3];
  const unsigned cache_bytes = (unsigned)in_sizes[1] * 2u;
  attn_split<<<NSPLIT * HKV, NTHR, 0, stream>>>((const uint16_t*)d_in[0], (const uint16_t*)d_in[1], (const uint16_t*)d_in[2],
                                                (const int*)d_in[3], (const int*)d_in[4], (float*)d_ws, nbps, cache_bytes);
  attn_combine<<<HQ, D, 0, stream>>>((const float*)d_ws, (__hip_bfloat16*)d_out);
}
```

```cpp
#include <hip/hip_runtime.h>
#include <hip/hip_bf16.h>
#include <stdint.h>

namespace {

constexpr int D = 256, PAGE = 16, HQ = 16, HKV = 8;
constexpr int NSPLIT = 32, WAVES = 8, NTHR = WAVES * 64;
constexpr int PAGE_BYTES = PAGE * HKV * D * 2;
constexpr int TOK_BYTES = HKV * D * 2;
constexpr int ROW_BYTES = D * 2;
constexpr int PSTRIDE = NSPLIT * WAVES;
constexpr float C2 = 0.0625f * 1.4426950408889634f;

using bf16x2 = __attribute__((ext_vector_type(2))) __bf16;
using u32x4 = __attribute__((ext_vector_type(4))) unsigned;
using f32x2 = __attribute__((ext_vector_type(2))) float;
using f32x4 = __attribute__((ext_vector_type(4))) float;

__device__ __forceinline__ float dot2(unsigned k, unsigned q, float acc) {
  return __builtin_amdgcn_fdot2_f32_bf16(__builtin_bit_cast(bf16x2, k), __builtin_bit_cast(bf16x2, q), acc, false);
}
template <int CTRL> __device__ __forceinline__ float dpp(float x) {
  return __builtin_bit_cast(float, __builtin_amdgcn_mov_dpp(__builtin_bit_cast(int, x), CTRL, 0xf, 0xf, true));
}
constexpr int XOR1 = 0xB1, XOR2 = 0x4E, XOR7 = 0x141, ROR8 = 0x128, ROR4 = 0x124, ROR2 = 0x122, BCAST = 0x150;

struct Pair { float a, b; };
__device__ __forceinline__ Pair swap16(float a, float b) {
  auto s = __builtin_amdgcn_permlane16_swap(__float_as_uint(a), __float_as_uint(b), false, false);
  return {__uint_as_float(s[0]), __uint_as_float(s[1])};
}
__device__ __forceinline__ Pair swap32(float a, float b) {
  auto s = __builtin_amdgcn_permlane32_swap(__float_as_uint(a), __float_as_uint(b), false, false);
  return {__uint_as_float(s[0]), __uint_as_float(s[1])};
}

#define STAMP(i)
#define STAMP_DECL
#define STAMP_FLUSH(wg, wave)
constexpr int AUX_NT = 2;

__device__ __forceinline__ void issue8(u32x4 (&buf)[8], const void* base, unsigned bytes, unsigned voff, unsigned soff) {
  const __amdgpu_buffer_rsrc_t r = __builtin_amdgcn_make_buffer_rsrc(const_cast<void*>(base), 0, bytes, 0x00020000);
  __builtin_amdgcn_sched_barrier(0);
#pragma unroll
  for (int i = 0; i < 8; ++i) buf[i] = __builtin_amdgcn_raw_buffer_load_b128(r, voff, soff + i * 2 * TOK_BYTES, AUX_NT);
  asm volatile("" ::: "memory");
  __builtin_amdgcn_sched_barrier(0);
}

struct State { float m, l; f32x2 acc[2][4]; };

__global__ void __launch_bounds__(NTHR) attn_split(const uint16_t* q, const uint16_t* kc,
                                                  const uint16_t* vc, const int* __restrict__ bt,
                                                  const int* __restrict__ lens, float* __restrict__ ws,
                                                  int nbps, unsigned cache_bytes) {
  __shared__ __attribute__((aligned(16))) float lds[WAVES * 2 * D + WAVES * 4];
  STAMP_DECL; STAMP(0);
  const int s = blockIdx.x & (NSPLIT - 1), h = blockIdx.x / NSPLIT;
  const int tid = threadIdx.x, lane = tid & 63, wave = __builtin_amdgcn_readfirstlane(tid >> 6);
  const int hw = lane >> 5, c = lane & 31;
  const int pg0 = s + NSPLIT * wave;
  const int btv = bt[min(pg0 + PSTRIDE * lane, nbps - 1)];
  u32x4 qv[2];
  qv[0] = *reinterpret_cast<const u32x4*>(q + (2 * h) * D + 8 * c);
  qv[1] = *reinterpret_cast<const u32x4*>(q + (2 * h + 1) * D + 8 * c);
  int len = lens[0];
  const int skv = nbps * PAGE;
  const bool uniform = len <= 0;
  len = (uniform || len > skv) ? skv : len;
  const int npages = (len + PAGE - 1) / PAGE;
  const int n = npages > pg0 ? (npages - pg0 + PSTRIDE - 1) / PSTRIDE : 0;
  const unsigned voff = hw * TOK_BYTES + c * 16;
  const unsigned hoff = h * ROW_BYTES;
  const int tok = 2 * ((lane >> 1) & 7) + hw;

  u32x4 KA[8], VA[8], KB[8], VB[8];
  auto soff_of = [&](int i) { return (unsigned)__builtin_amdgcn_readlane(btv, i & 63) * (unsigned)PAGE_BYTES + hoff; };
  auto bytes_of = [&](int i) { return (unsigned)i < (unsigned)n ? cache_bytes : 0u; };
  const int i0 = -(n & 1);
  issue8(KA, kc, bytes_of(i0), voff, soff_of(i0));
  issue8(VA, vc, bytes_of(i0), voff, soff_of(i0));
  issue8(KB, kc, bytes_of(i0 + 1), voff, soff_of(i0 + 1));
  issue8(VB, vc, bytes_of(i0 + 1), voff, soff_of(i0 + 1));

  STAMP(1);
  float m = -INFINITY, l = 0.f;
  f32x2 acc[2][4];
#pragma unroll
  for (int g = 0; g < 2; ++g)
#pragma unroll
    for (int k = 0; k < 4; ++k) acc[g][k] = f32x2{0.f, 0.f};

  auto step = [&](u32x4 (&K)[8], u32x4 (&V)[8], int i) {
    float y[8];
#pragma unroll
    for (int t = 0; t < 8; ++t) {
      float x0 = 0.f, x1 = 0.f;
#pragma unroll
      for (int k = 0; k < 4; ++k) { x0 = dot2(K[t][k], qv[0][k], x0); x1 = dot2(K[t][k], qv[1][k], x1); }
      const Pair p = swap16(x0, x1);
      y[t] = p.a + p.b;
    }
    const unsigned so = soff_of(i + 2), by = bytes_of(i + 2);
    issue8(K, kc, by, voff, so);
#pragma unroll
    for (int j = 0; j < 4; ++j) { const float t1 = y[j] + dpp<ROR8>(y[j]), t2 = y[j + 4] + dpp<ROR8>(y[j + 4]); y[j] = (lane & 8) ? t2 : t1; }
#pragma unroll
    for (int j = 0; j < 2; ++j) { const float t1 = y[j] + dpp<XOR7>(y[j]), t2 = y[j + 2] + dpp<XOR7>(y[j + 2]); y[j] = (lane & 4) ? t2 : t1; }
    { const float t1 = y[0] + dpp<XOR2>(y[0]), t2 = y[1] + dpp<XOR2>(y[1]); y[0] = (lane & 2) ? t2 : t1; }
    float sc = y[0] + dpp<XOR1>(y[0]);
    const int rem = i < 0 ? 0 : len - (pg0 + PSTRIDE * i) * PAGE;
    sc = uniform ? 0.f : sc * C2;
    sc = tok < rem ? sc : -INFINITY;
    float pm = fmaxf(sc, dpp<ROR8>(sc)); pm = fmaxf(pm, dpp<ROR4>(pm)); pm = fmaxf(pm, dpp<ROR2>(pm));
    const float mn = fmaxf(m, pm), msub = (mn == -INFINITY) ? 0.f : mn;
    const float alpha = __builtin_amdgcn_exp2f(m - msub), p = __builtin_amdgcn_exp2f(sc - msub);
    l = fmaf(l, alpha, p); m = mn;
    const Pair al = swap16(alpha, alpha), pp = swap16(p, p);
#pragma unroll
    for (int k = 0; k < 4; ++k) { acc[0][k] *= al.a; acc[1][k] *= al.b; }
#define PV(T) { const float p0 = dpp<BCAST + 2 * T>(pp.a), p1 = dpp<BCAST + 2 * T>(pp.b); \
      _Pragma("unroll") for (int k = 0; k < 4; ++k) { \
        const f32x2 vf = {__builtin_bit_cast(float, V[T][k] << 16), __builtin_bit_cast(float, V[T][k] & 0xffff0000u)}; \
        acc[0][k] += f32x2{p0, p0} * vf; acc[1][k] += f32x2{p1, p1} * vf; } }
    PV(0) PV(1) PV(2) PV(3) PV(4) PV(5) PV(6) PV(7)
#undef PV
    issue8(V, vc, by, voff, so);
  };

  for (int i = i0; i < n; i += 2) {
    step(KA, VA, i);
    step(KB, VB, i + 1);
  }

  STAMP(2);
  l += dpp<ROR8>(l); l += dpp<ROR4>(l); l += dpp<ROR2>(l);
  { const Pair mm = swap32(m, m); const float M = fmaxf(mm.a, mm.b), Ms = (M == -INFINITY) ? 0.f : M;
    const float w = __builtin_amdgcn_exp2f(m - Ms);
    const Pair ll = swap32(l * w, l * w); l = ll.a + ll.b; m = M;
    const Pair ww = swap16(w, w);
    float o[8];
#pragma unroll
    for (int k = 0; k < 4; ++k) {
      const Pair x = swap32(acc[0][k].x * ww.a, acc[1][k].x * ww.b), y2 = swap32(acc[0][k].y * ww.a, acc[1][k].y * ww.b);
      o[2 * k] = x.a + x.b; o[2 * k + 1] = y2.a + y2.b; }
    float* rec = lds + (wave * 2 + hw) * D + 8 * c;
    *reinterpret_cast<f32x4*>(rec) = f32x4{o[0], o[1], o[2], o[3]};
    *reinterpret_cast<f32x4*>(rec + 4) = f32x4{o[4], o[5], o[6], o[7]};
    if ((lane & 47) == 0) { float* ml = lds + WAVES * 2 * D + wave * 4 + 2 * (lane >> 4); ml[0] = m; ml[1] = l; }
  }
  __syncthreads();
  { const int g = tid >> 8, d = tid & 255;
    const float* ml = lds + WAVES * 2 * D + 2 * g;
    float M = -INFINITY;
#pragma unroll
    for (int w = 0; w < WAVES; ++w) M = fmaxf(M, ml[4 * w]);
    const float Ms = (M == -INFINITY) ? 0.f : M;
    float L = 0.f, o = 0.f;
#pragma unroll
    for (int w = 0; w < WAVES; ++w) { const float wt = __builtin_amdgcn_exp2f(ml[4 * w] - Ms);
      L = fmaf(wt, ml[4 * w + 1], L); o = fmaf(wt, lds[(w * 2 + g) * D + d], o); }
    const int row = 2 * h + g;
    ws[(row * NSPLIT + s) * D + d] = o;
    if (d == 0) { float* pml = ws + HQ * NSPLIT * D + (row * NSPLIT + s) * 2; pml[0] = M; pml[1] = L; }
  }
  STAMP(3); STAMP_FLUSH(blockIdx.x, wave);
}

__global__ void __launch_bounds__(D) attn_combine(const float* __restrict__ ws, __hip_bfloat16* __restrict__ out) {
  STAMP_DECL; STAMP(4);
  const int row = blockIdx.x, d = threadIdx.x;
  const f32x2* pml = reinterpret_cast<const f32x2*>(ws + HQ * NSPLIT * D) + row * NSPLIT;
  const float* po = ws + row * NSPLIT * D + d;
  float a[NSPLIT]; f32x2 ml[NSPLIT];
#pragma unroll
  for (int s = 0; s < NSPLIT; ++s) { a[s] = po[s * D]; ml[s] = pml[s]; }
  float M = -INFINITY;
#pragma unroll
  for (int s = 0; s < NSPLIT; ++s) M = fmaxf(M, ml[s].x);
  float L = 0.f, o = 0.f;
#pragma unroll
  for (int s = 0; s < NSPLIT; ++s) { const float w = __builtin_amdgcn_exp2f(ml[s].x - M); L = fmaf(w, ml[s].y, L); o = fmaf(w, a[s], o); }
  out[row * D + d] = __float2bfloat16(o / L);
  STAMP(5); STAMP_FLUSH(512 + blockIdx.x, threadIdx.x >> 6);
}

}

extern "C" void kernel_launch(void* const* d_in, const int* in_sizes, int n_in, void* d_out, int out_size,
                              void* d_ws, size_t ws_size, hipStream_t stream) {
  const int nbps = in_sizes[3];
  const unsigned cache_bytes = (unsigned)in_sizes[1] * 2u;
  attn_split<<<NSPLIT * HKV, NTHR, 0, stream>>>((const uint16_t*)d_in[0], (const uint16_t*)d_in[1], (const uint16_t*)d_in[2],
                                                (const int*)d_in[3], (const int*)d_in[4], (float*)d_ws, nbps, cache_bytes);
  attn_combine<<<HQ, D, 0, stream>>>((const float*)d_ws, (__hip_bfloat16*)d_out);
}
```

```cpp
#include <hip/hip_runtime.h>
#include <hip/hip_bf16.h>
#include <stdint.h>

#define WIN 11

namespace {

constexpr int D = 256, PAGE = 16, HKV = 8, NWG = 256;
constexpr int NBPS = 4096, NUM_PHYS = NBPS + 4;
constexpr int WAVES = 2, NTHR = WAVES * 64;
constexpr int NSPL = NWG / HKV;
constexpr int RECS = WAVES * 2;
constexpr int NWV = NSPL * WAVES;
constexpr int NL = 4;
constexpr int PAGE_BYTES = PAGE * HKV * D * 2, TOK_BYTES = HKV * D * 2, ROW_BYTES = D * 2;
constexpr unsigned CACHE_BYTES = (unsigned)NUM_PHYS * PAGE_BYTES;
constexpr float C2 = 0.0625f * 1.4426950408889634f;
constexpr unsigned WS_DATA = 4096, WS_BYTES = WS_DATA + HKV * NSPL * NSPL * 128;
constexpr int AUX_NT = 2, AUX_SC1 = 16;
constexpr unsigned SPIN_CAP = 1u << 16;

using bf16x2 = __attribute__((ext_vector_type(2))) __bf16;
using u32x4 = __attribute__((ext_vector_type(4))) unsigned;
using f32x2 = __attribute__((ext_vector_type(2))) float;
using f32x4 = __attribute__((ext_vector_type(4))) float;
using rsrc_t = __amdgpu_buffer_rsrc_t;
using const_int = const __attribute__((address_space(4))) int;

#define STAMP(i)
#define STAMP_DECL
#define STAMP_FLUSH(wg, wave)

__device__ __forceinline__ float dot2(unsigned k, unsigned q, float acc) {
  return __builtin_amdgcn_fdot2_f32_bf16(__builtin_bit_cast(bf16x2, k), __builtin_bit_cast(bf16x2, q), acc, false);
}
template <int CTRL> __device__ __forceinline__ float dpp(float x) {
  return __builtin_bit_cast(float, __builtin_amdgcn_mov_dpp(__builtin_bit_cast(int, x), CTRL, 0xf, 0xf, true));
}
constexpr int XOR1 = 0xB1, XOR2 = 0x4E, XOR7 = 0x141, ROR8 = 0x128, ROR4 = 0x124, EVEN = 0xA0, ODD = 0xF5, BCAST = 0x150;

struct Pair { float a, b; };
__device__ __forceinline__ Pair swap16(float a, float b) {
  auto s = __builtin_amdgcn_permlane16_swap(__float_as_uint(a), __float_as_uint(b), false, false);
  return {__uint_as_float(s[0]), __uint_as_float(s[1])};
}
__device__ __forceinline__ Pair swap32(float a, float b) {
  auto s = __builtin_amdgcn_permlane32_swap(__float_as_uint(a), __float_as_uint(b), false, false);
  return {__uint_as_float(s[0]), __uint_as_float(s[1])};
}
__device__ __forceinline__ float sum_8apart(float x) { x += dpp<ROR8>(x); Pair p = swap16(x, x); x = p.a + p.b; p = swap32(x, x); return p.a + p.b; }
__device__ __forceinline__ float max_8apart(float x) { x = fmaxf(x, dpp<ROR8>(x)); Pair p = swap16(x, x); x = fmaxf(p.a, p.b); p = swap32(x, x); return fmaxf(p.a, p.b); }
__device__ __forceinline__ float safe_max(float m) { return m == -INFINITY ? 0.f : m; }
__device__ __forceinline__ rsrc_t rsrc(const void* base, unsigned bytes) {
  return __builtin_amdgcn_make_buffer_rsrc(const_cast<void*>(base), 0, bytes, 0x00020000);
}
template <bool THROTTLE> __device__ __forceinline__ void fetch(u32x4& dst, rsrc_t r, unsigned voff, unsigned soff) {
  if (THROTTLE) asm volatile("s_waitcnt vmcnt(%0)" :: "i"(WIN - 1) : "memory");
  __builtin_amdgcn_sched_barrier(0);
  dst = __builtin_amdgcn_raw_buffer_load_b128(r, voff, soff, AUX_NT);
  asm volatile("" ::: "memory");
  __builtin_amdgcn_sched_barrier(0);
}
template <int T> __device__ __forceinline__ void pv_step(f32x2 (&acc)[2][4], const u32x4& v, float pa, float pb) {
  const float p0 = dpp<BCAST + 4 * T>(pa), p1 = dpp<BCAST + 4 * T>(pb);
#pragma unroll
  for (int k = 0; k < 4; ++k) {
    const f32x2 vf = {__builtin_bit_cast(float, v[k] << 16), __builtin_bit_cast(float, v[k] & 0xffff0000u)};
    acc[0][k] += f32x2{p0, p0} * vf; acc[1][k] += f32x2{p1, p1} * vf;
  }
  asm volatile("" : "+v"(acc[0][0]), "+v"(acc[0][1]), "+v"(acc[0][2]), "+v"(acc[0][3]),
                    "+v"(acc[1][0]), "+v"(acc[1][1]), "+v"(acc[1][2]), "+v"(acc[1][3]));
}

__global__ void __launch_bounds__(NTHR, 1) attn_decode(const int* bt, const uint16_t* kc, const uint16_t* vc, const int* lens,
                                                      unsigned char* ws, const uint16_t* q, unsigned* out) {
  __shared__ __attribute__((aligned(16))) float lds[RECS * 2 * D + RECS * 2 * 2];
  STAMP_DECL; STAMP(0);
  const int s = blockIdx.x % NSPL, head = blockIdx.x / NSPL;
  const int tid = threadIdx.x, lane = tid & 63, wave = __builtin_amdgcn_readfirstlane(tid >> 6);
  const int hw = lane >> 5, c = lane & 31, idx = (lane & 15) >> 2;
  const int w = s + NSPL * wave;
  const unsigned page0 = ((const_int*)bt)[w];
  __builtin_amdgcn_sched_barrier(0);
  const int btv = bt[w + NWV * lane];
  const rsrc_t wsr = rsrc(ws, WS_BYTES), kr = rsrc(kc, CACHE_BYTES), vr = rsrc(vc, CACHE_BYTES);
  const int lenv = __builtin_amdgcn_raw_buffer_load_b32(rsrc(lens, 4), 0, 0, 0);
  const unsigned epoch = __builtin_amdgcn_raw_buffer_load_b32(wsr, 0, head * 256, AUX_SC1);
  u32x4 qv[2];
  qv[0] = *reinterpret_cast<const u32x4*>(q + head * 2 * D + 8 * c);
  qv[1] = *reinterpret_cast<const u32x4*>(q + head * 2 * D + D + 8 * c);
  const unsigned voff[2] = {hw * 9u * TOK_BYTES + c * 16u, TOK_BYTES + hw * 7u * TOK_BYTES + c * 16u};
  const unsigned hoff = head * ROW_BYTES;

  u32x4 KA[NL], VA[NL], KB[NL], VB[NL];
  { const unsigned so = page0 * (unsigned)PAGE_BYTES + hoff;
    STAMP(1);
#pragma unroll
    for (int t = 0; t < NL; ++t) fetch<false>(KA[t], kr, voff[t & 1], so + (t >> 1) * 2 * TOK_BYTES);
#pragma unroll
    for (int t = 0; t < NL; ++t) fetch<false>(VA[t], vr, voff[t & 1], so + (t >> 1) * 2 * TOK_BYTES);
#pragma unroll
    for (int t = 0; t < NL; ++t) fetch<false>(KB[t], kr, voff[t & 1], so + (2 + (t >> 1)) * 2 * TOK_BYTES);
#pragma unroll
    for (int t = 0; t < NL; ++t) fetch<false>(VB[t], vr, voff[t & 1], so + (2 + (t >> 1)) * 2 * TOK_BYTES); }
  STAMP(2);

  int len = __builtin_amdgcn_readfirstlane(lenv);
  const bool uniform = len <= 0;
  len = (uniform || len > NBPS * PAGE) ? NBPS * PAGE : len;
  const int npages = (len + PAGE - 1) / PAGE;
  const int n = (npages - w + NWV - 1) / NWV;
  const int tok = idx ^ (hw * 9);

  float m = -INFINITY, l = 0.f;
  f32x2 acc[2][4];
#pragma unroll
  for (int g = 0; g < 2; ++g)
#pragma unroll
    for (int k = 0; k < 4; ++k) acc[g][k] = f32x2{0.f, 0.f};

  auto step = [&](u32x4 (&K)[NL], u32x4 (&V)[NL], unsigned so0, bool live) {
    const unsigned so1 = so0 + 2 * TOK_BYTES;
    float y[NL];
#pragma unroll
    for (int t = 0; t < NL; ++t) {
      float x0 = 0.f, x1 = 0.f;
#pragma unroll
      for (int k = 0; k < 4; ++k) { x0 = dot2(K[t][k], qv[0][k], x0); x1 = dot2(K[t][k], qv[1][k], x1); }
      asm volatile("" : "+v"(x0), "+v"(x1));
      fetch<true>(K[t], kr, voff[t & 1], t < 2 ? so0 : so1);
      const Pair p = swap16(x0, x1);
      y[t] = p.a + p.b;
    }
#pragma unroll
    for (int j = 0; j < 2; ++j) { const float t1 = y[j] + dpp<ROR8>(y[j]), t2 = y[j + 2] + dpp<ROR8>(y[j + 2]); y[j] = (lane & 8) ? t2 : t1; }
    { const float t1 = y[0] + dpp<XOR7>(y[0]), t2 = y[1] + dpp<XOR7>(y[1]); y[0] = (lane & 4) ? t2 : t1; }
    y[0] += dpp<XOR2>(y[0]);
    float sc = y[0] + dpp<XOR1>(y[0]);
    sc = uniform ? 0.f : sc * C2;
    sc = live ? sc : -INFINITY;
    float pm = fmaxf(sc, dpp<ROR8>(sc)); pm = fmaxf(pm, dpp<ROR4>(pm));
    const float mn = fmaxf(m, pm), msub = safe_max(mn);
    const float alpha = __builtin_amdgcn_exp2f(m - msub), p = __builtin_amdgcn_exp2f(sc - msub);
    l = fmaf(l, alpha, p); m = mn;
    const Pair al = swap16(alpha, alpha), pp = swap16(p, p);
#pragma unroll
    for (int k = 0; k < 4; ++k) { acc[0][k] *= al.a; acc[1][k] *= al.b; }
    pv_step<0>(acc, V[0], pp.a, pp.b); fetch<true>(V[0], vr, voff[0], so0);
    pv_step<1>(acc, V[1], pp.a, pp.b); fetch<true>(V[1], vr, voff[1], so0);
    pv_step<2>(acc, V[2], pp.a, pp.b); fetch<true>(V[2], vr, voff[0], so1);
    pv_step<3>(acc, V[3], pp.a, pp.b); fetch<true>(V[3], vr, voff[1], so1);
  };
#pragma nounroll
  for (int i = 0; i < n; ++i) {
    const unsigned so = i + 1 < n ? (unsigned)__builtin_amdgcn_readlane(btv, i + 1) * (unsigned)PAGE_BYTES + hoff : CACHE_BYTES;
    const int rem = len - (w + NWV * i) * PAGE;
    step(KA, VA, so, tok < rem);
    step(KB, VB, so + NL * TOK_BYTES, tok + NL < rem);
  }
  STAMP(3);

  constexpr int NK = NSPL / 8;
  const unsigned tag = (epoch + 1u) & 0xffffu;
  float* lml = lds + RECS * 2 * D;
  unsigned slot = WS_DATA + ((unsigned)(head * NSPL + (tid >> 3)) * NSPL + s) * 128u + (tid & 7) * 16u;
  unsigned doff = WS_DATA + blockIdx.x * NSPL * 128u + lane * 16u;
#pragma unroll
  for (int g = 0; g < 2; ++g) {
    float* dst = lds + ((wave * 2 + hw) * 2 + g) * D + 8 * c;
    *reinterpret_cast<f32x4*>(dst) = f32x4{acc[g][0].x, acc[g][0].y, acc[g][1].x, acc[g][1].y};
    *reinterpret_cast<f32x4*>(dst + 4) = f32x4{acc[g][2].x, acc[g][2].y, acc[g][3].x, acc[g][3].y};
  }
  l += dpp<ROR8>(l); l += dpp<ROR4>(l);
  if ((lane & 15) == 0) *reinterpret_cast<f32x2*>(lml + (wave * 4 + (lane >> 4)) * 2) = f32x2{m, l};
  asm volatile("" : "+v"(slot), "+v"(doff));
  __syncthreads();
#pragma unroll
  for (int g = 0; g < 2; ++g) {
    f32x2 ml[RECS], a[RECS];
#pragma unroll
    for (int r = 0; r < RECS; ++r) ml[r] = *reinterpret_cast<const f32x2*>(lml + (r * 2 + g) * 2);
#pragma unroll
    for (int r = 0; r < RECS; ++r) a[r] = *reinterpret_cast<const f32x2*>(lds + (r * 2 + g) * D + 2 * tid);
    float M = ml[0].x;
#pragma unroll
    for (int r = 1; r < RECS; ++r) M = fmaxf(M, ml[r].x);
    const float Ms = safe_max(M);
    float L = 0.f; f32x2 o = {0.f, 0.f};
#pragma unroll
    for (int r = 0; r < RECS; ++r) { const float wt = __builtin_amdgcn_exp2f(ml[r].x - Ms); L = fmaf(wt, ml[r].y, L); o += f32x2{wt, wt} * a[r]; }
    const unsigned x = __float_as_uint((lane & 1) ? L : M);
    __builtin_amdgcn_raw_buffer_store_b128(u32x4{__float_as_uint(o.x), (x << 16) | tag, __float_as_uint(o.y), (x & 0xffff0000u) | tag},
                                           wsr, slot, g * (NSPL / 2) * NSPL * 128, AUX_SC1);
  }
  STAMP(4);
  if (wave == WAVES - 1) {
    u32x4 gr[NK];
    bool pend[NK];
#pragma unroll
    for (int k = 0; k < NK; ++k) { gr[k] = u32x4{0u, 0u, 0u, 0u}; pend[k] = true; }
#pragma nounroll
    for (unsigned spin = 0; spin < SPIN_CAP; ++spin) {
      u32x4 dv[NK];
#pragma unroll
      for (int k = 0; k < NK; ++k)
        dv[k] = __builtin_amdgcn_raw_buffer_load_b128(wsr, pend[k] ? doff + k * 1024u : WS_BYTES, 0, AUX_SC1);
      asm volatile("" ::: "memory");
      __builtin_amdgcn_sched_barrier(0);
      bool any = false;
#pragma unroll
      for (int k = 0; k < NK; ++k) {
#pragma unroll
        for (int j = 0; j < 4; ++j) gr[k][j] = pend[k] ? dv[k][j] : gr[k][j];
        pend[k] = pend[k] & ((((dv[k].y ^ tag) | (dv[k].w ^ tag)) & 0xffffu) != 0u);
        any |= pend[k];
      }
      if (!__any(any)) break;
      __builtin_amdgcn_s_sleep(1);
    }
    STAMP(5);
    float x[NK], pm[NK];
#pragma unroll
    for (int k = 0; k < NK; ++k) { x[k] = __uint_as_float((gr[k].y >> 16) | (gr[k].w & 0xffff0000u)); pm[k] = dpp<EVEN>(x[k]); }
    float M = pm[0];
#pragma unroll
    for (int k = 1; k < NK; ++k) M = fmaxf(M, pm[k]);
    M = max_8apart(M);
    float L = 0.f, o0 = 0.f, o1 = 0.f;
#pragma unroll
    for (int k = 0; k < NK; ++k) {
      const float wt = __builtin_amdgcn_exp2f(pm[k] - M);
      L = fmaf(wt, dpp<ODD>(x[k]), L); o0 = fmaf(wt, __uint_as_float(gr[k].x), o0); o1 = fmaf(wt, __uint_as_float(gr[k].z), o1);
    }
    L = sum_8apart(L); o0 = sum_8apart(o0); o1 = sum_8apart(o1);
    const float inv = __builtin_amdgcn_rcpf(L);
    const bf16x2 r = {(__bf16)(o0 * inv), (__bf16)(o1 * inv)};
    if (lane < 8) __hip_atomic_store(out + blockIdx.x * 8 + lane, __builtin_bit_cast(unsigned, r), __ATOMIC_RELAXED, __HIP_MEMORY_SCOPE_AGENT);
    if (s == 0 && lane == 0) __builtin_amdgcn_raw_buffer_store_b32(epoch + 1u, wsr, 0, head * 256, AUX_SC1);
    STAMP(6);
  }
  STAMP_FLUSH(blockIdx.x, wave);
}

}

extern "C" void kernel_launch(void* const* d_in, const int* in_sizes, int n_in, void* d_out, int out_size,
                              void* d_ws, size_t ws_size, hipStream_t stream) {
  attn_decode<<<NWG, NTHR, 0, stream>>>((const int*)d_in[3], (const uint16_t*)d_in[1], (const uint16_t*)d_in[2], (const int*)d_in[4],
                                        (unsigned char*)d_ws, (const uint16_t*)d_in[0], (unsigned*)d_out);
}
```

```cpp
#include <hip/hip_runtime.h>
#include <hip/hip_bf16.h>
#include <stdint.h>

namespace {

constexpr int D = 256, PAGE = 16, HKV = 8, NWG = 256;
constexpr int NBPS = 4096, NUM_PHYS = NBPS + 4;
constexpr int WAVES = 2, NTHR = WAVES * 64;
constexpr int NSPL = NWG / HKV;
constexpr int RECS = WAVES * 2;
constexpr int NWV = NSPL * WAVES;
constexpr int WIN = 11;
constexpr int NL = 8;
constexpr int MAXN = NBPS / NWV;
constexpr int PAGE_BYTES = PAGE * HKV * D * 2, TOK_BYTES = HKV * D * 2, ROW_BYTES = D * 2;
constexpr unsigned CACHE_BYTES = (unsigned)NUM_PHYS * PAGE_BYTES;
constexpr float C2 = 0.0625f * 1.4426950408889634f;
constexpr unsigned WS_DATA = 4096, WS_BYTES = WS_DATA + HKV * NSPL * NSPL * 128;
constexpr int AUX_NT = 2, AUX_SC1 = 16;
constexpr unsigned SPIN_CAP = 1u << 20;

using bf16x2 = __attribute__((ext_vector_type(2))) __bf16;
using u32x4 = __attribute__((ext_vector_type(4))) unsigned;
using f32x2 = __attribute__((ext_vector_type(2))) float;
using f32x4 = __attribute__((ext_vector_type(4))) float;
using rsrc_t = __amdgpu_buffer_rsrc_t;
using const_int = const __attribute__((address_space(4))) int;

__device__ __forceinline__ float dot2(unsigned k, unsigned q, float acc) {
  return __builtin_amdgcn_fdot2_f32_bf16(__builtin_bit_cast(bf16x2, k), __builtin_bit_cast(bf16x2, q), acc, false);
}
template <int CTRL> __device__ __forceinline__ float dpp(float x) {
  return __builtin_bit_cast(float, __builtin_amdgcn_mov_dpp(__builtin_bit_cast(int, x), CTRL, 0xf, 0xf, true));
}
constexpr int XOR1 = 0xB1, XOR2 = 0x4E, XOR7 = 0x141, ROR8 = 0x128, ROR4 = 0x124, ROR2 = 0x122, EVEN = 0xA0, ODD = 0xF5, BCAST = 0x150;

struct Pair { float a, b; };
__device__ __forceinline__ Pair swap16(float a, float b) {
  auto s = __builtin_amdgcn_permlane16_swap(__float_as_uint(a), __float_as_uint(b), false, false);
  return {__uint_as_float(s[0]), __uint_as_float(s[1])};
}
__device__ __forceinline__ Pair swap32(float a, float b) {
  auto s = __builtin_amdgcn_permlane32_swap(__float_as_uint(a), __float_as_uint(b), false, false);
  return {__uint_as_float(s[0]), __uint_as_float(s[1])};
}
__device__ __forceinline__ float sum_8apart(float x) { x += dpp<ROR8>(x); Pair p = swap16(x, x); x = p.a + p.b; p = swap32(x, x); return p.a + p.b; }
__device__ __forceinline__ float max_8apart(float x) { x = fmaxf(x, dpp<ROR8>(x)); Pair p = swap16(x, x); x = fmaxf(p.a, p.b); p = swap32(x, x); return fmaxf(p.a, p.b); }
template <int CTRL> __device__ __forceinline__ unsigned dppu(unsigned x) { return __builtin_amdgcn_mov_dpp(x, CTRL, 0xf, 0xf, true); }
__device__ __forceinline__ unsigned mix(unsigned x) { x ^= x >> 16; x *= 0x7feb352du; x ^= x >> 15; x *= 0x846ca68bu; return x ^ (x >> 16); }
__device__ __forceinline__ float safe_max(float m) { return m == -INFINITY ? 0.f : m; }
__device__ __forceinline__ rsrc_t rsrc(const void* base, unsigned bytes) {
  return __builtin_amdgcn_make_buffer_rsrc(const_cast<void*>(base), 0, bytes, 0x00020000);
}
template <bool THROTTLE> __device__ __forceinline__ void fetch(u32x4& dst, rsrc_t r, unsigned voff, unsigned soff) {
  if (THROTTLE) asm volatile("s_waitcnt vmcnt(%0)" :: "i"(WIN - 1) : "memory");
  __builtin_amdgcn_sched_barrier(0);
  dst = __builtin_amdgcn_raw_buffer_load_b128(r, voff, soff, AUX_NT);
  asm volatile("" ::: "memory");
  __builtin_amdgcn_sched_barrier(0);
}
template <int T> __device__ __forceinline__ void pv_step(f32x2 (&acc)[2][4], const u32x4& v, float pa, float pb) {
  const float p0 = dpp<BCAST + 2 * T>(pa), p1 = dpp<BCAST + 2 * T>(pb);
#pragma unroll
  for (int k = 0; k < 4; ++k) {
    const f32x2 vf = {__builtin_bit_cast(float, v[k] << 16), __builtin_bit_cast(float, v[k] & 0xffff0000u)};
    acc[0][k] += f32x2{p0, p0} * vf; acc[1][k] += f32x2{p1, p1} * vf;
  }
  asm volatile("" : "+v"(acc[0][0]), "+v"(acc[0][1]), "+v"(acc[0][2]), "+v"(acc[0][3]),
                    "+v"(acc[1][0]), "+v"(acc[1][1]), "+v"(acc[1][2]), "+v"(acc[1][3]));
}

__global__ void __launch_bounds__(NTHR, 1) attn_decode(const int* bt, const uint16_t* kc, const uint16_t* vc, const int* lens,
                                                      unsigned char* ws, const uint16_t* q, unsigned* out) {
  __shared__ __attribute__((aligned(16))) float lds[RECS * 2 * D + RECS * 2 * 2 + WAVES * (MAXN + 2) * 64];
  const int s = blockIdx.x % NSPL, head = blockIdx.x / NSPL;
  const int tid = threadIdx.x, lane = tid & 63, wave = __builtin_amdgcn_readfirstlane(tid >> 6);
  const int hw = lane >> 5, c = lane & 31;
  const int w = s + NSPL * wave;
  const unsigned page0 = ((const_int*)bt)[w], page1 = ((const_int*)bt)[w + NWV];
  __builtin_amdgcn_sched_barrier(0);
  const int btv = bt[w + NWV * lane];
  const rsrc_t wsr = rsrc(ws, WS_BYTES);
  const int lenv = __builtin_amdgcn_raw_buffer_load_b32(rsrc(lens, 4), 0, 0, 0);
  const unsigned epochv = __builtin_amdgcn_raw_buffer_load_b32(wsr, 0, head * 256, AUX_SC1);
  u32x4 qv[2];
  qv[0] = *reinterpret_cast<const u32x4*>(q + head * 2 * D + 8 * c);
  qv[1] = *reinterpret_cast<const u32x4*>(q + head * 2 * D + D + 8 * c);
  const unsigned voff[2] = {hw * 9u * TOK_BYTES + c * 16u, TOK_BYTES + hw * 7u * TOK_BYTES + c * 16u};
  const unsigned hoff = head * ROW_BYTES;

  u32x4 A[NL], B[NL];
  { const rsrc_t kr = rsrc(kc, CACHE_BYTES);
#pragma unroll
    for (int t = 0; t < NL; ++t) fetch<false>(A[t], kr, voff[t & 1], page0 * (unsigned)PAGE_BYTES + hoff + (t >> 1) * 2 * TOK_BYTES);
#pragma unroll
    for (int t = 0; t < NL; ++t) fetch<false>(B[t], kr, voff[t & 1], page1 * (unsigned)PAGE_BYTES + hoff + (t >> 1) * 2 * TOK_BYTES); }

  int len = __builtin_amdgcn_readfirstlane(lenv);
  const bool uniform = len <= 0;
  len = (uniform || len > NBPS * PAGE) ? NBPS * PAGE : len;
  const int npages = (len + PAGE - 1) / PAGE;
  const int n = (npages - w + NWV - 1) / NWV;
  const unsigned epoch = __builtin_amdgcn_readfirstlane(epochv);
  unsigned tag = mix(mix(epoch) + (lane & 7) * 0x9e3779b9u + 1u);
  unsigned slot = WS_DATA + ((unsigned)(head * NSPL + (tid >> 3)) * NSPL + s) * 128u + (tid & 7) * 16u;
  unsigned doff = WS_DATA + blockIdx.x * NSPL * 128u + lane * 16u;
  asm volatile("" : "+v"(slot), "+v"(doff), "+v"(tag));
  const int tok = ((lane >> 1) & 7) ^ (hw * 9);
  float* const sco = lds + RECS * 2 * D + RECS * 2 * 2 + wave * (MAXN + 2) * 64 + lane;
  auto block = [&](int i) { return i < n ? (unsigned)__builtin_amdgcn_readlane(btv, i & 63) * (unsigned)PAGE_BYTES + hoff : CACHE_BYTES; };

  float m = -INFINITY;
  auto qk = [&](u32x4 (&K)[NL], rsrc_t r, unsigned so, int i) {
    float y[NL];
#pragma unroll
    for (int t = 0; t < NL; ++t) {
      float x0 = 0.f, x1 = 0.f;
#pragma unroll
      for (int k = 0; k < 4; ++k) { x0 = dot2(K[t][k], qv[0][k], x0); x1 = dot2(K[t][k], qv[1][k], x1); }
      asm volatile("" : "+v"(x0), "+v"(x1));
      fetch<true>(K[t], r, voff[t & 1], so + (t >> 1) * 2 * TOK_BYTES);
      const Pair p = swap16(x0, x1);
      y[t] = p.a + p.b;
    }
#pragma unroll
    for (int j = 0; j < 4; ++j) { const float t1 = y[j] + dpp<ROR8>(y[j]), t2 = y[j + 4] + dpp<ROR8>(y[j + 4]); y[j] = (lane & 8) ? t2 : t1; }
#pragma unroll
    for (int j = 0; j < 2; ++j) { const float t1 = y[j] + dpp<XOR7>(y[j]), t2 = y[j + 2] + dpp<XOR7>(y[j + 2]); y[j] = (lane & 4) ? t2 : t1; }
    { const float t1 = y[0] + dpp<XOR2>(y[0]), t2 = y[1] + dpp<XOR2>(y[1]); y[0] = (lane & 2) ? t2 : t1; }
    float sc = y[0] + dpp<XOR1>(y[0]);
    sc = uniform ? 0.f : sc * C2;
    sc = tok < len - (w + NWV * i) * PAGE ? sc : -INFINITY;
    m = fmaxf(m, sc);
    sco[i * 64] = sc;
  };
#pragma nounroll
  for (int i = 0; i < n; i += 2) {
    const bool more = i + 2 < n;
    const rsrc_t r = rsrc(more ? kc : vc, CACHE_BYTES);
    qk(A, r, block(more ? i + 2 : 0), i);
    qk(B, r, block(more ? i + 3 : 1), i + 1);
  }
  m = fmaxf(m, dpp<ROR8>(m)); m = fmaxf(m, dpp<ROR4>(m)); m = fmaxf(m, dpp<ROR2>(m));
  const float msub = safe_max(m);

  float l = 0.f;
  f32x2 acc[2][4];
#pragma unroll
  for (int g = 0; g < 2; ++g)
#pragma unroll
    for (int k = 0; k < 4; ++k) acc[g][k] = f32x2{0.f, 0.f};
  const rsrc_t vr = rsrc(vc, CACHE_BYTES);
  auto pv = [&](u32x4 (&V)[NL], unsigned so, float sc) {
    const float p = __builtin_amdgcn_exp2f(sc - msub);
    l += p;
    const Pair pp = swap16(p, p);
#define PV(T) pv_step<T>(acc, V[T], pp.a, pp.b); fetch<true>(V[T], vr, voff[T & 1], so + (T >> 1) * 2 * TOK_BYTES);
    PV(0) PV(1) PV(2) PV(3) PV(4) PV(5) PV(6) PV(7)
#undef PV
  };
  float sa = sco[0], sb = sco[64];
#pragma nounroll
  for (int i = 0; i < n; i += 2) {
    const float ca = sa, cb = sb;
    sa = sco[(i + 2) * 64]; sb = sco[(i + 3) * 64];
    pv(A, block(i + 2), ca);
    pv(B, block(i + 3), cb);
  }

  constexpr int NK = NSPL / 8;
  float* lml = lds + RECS * 2 * D;
#pragma unroll
  for (int g = 0; g < 2; ++g) {
    float* dst = lds + ((wave * 2 + hw) * 2 + g) * D + 8 * c;
    *reinterpret_cast<f32x4*>(dst) = f32x4{acc[g][0].x, acc[g][0].y, acc[g][1].x, acc[g][1].y};
    *reinterpret_cast<f32x4*>(dst + 4) = f32x4{acc[g][2].x, acc[g][2].y, acc[g][3].x, acc[g][3].y};
  }
  l += dpp<ROR8>(l); l += dpp<ROR4>(l); l += dpp<ROR2>(l);
  if ((lane & 15) == 0) *reinterpret_cast<f32x2*>(lml + (wave * 4 + (lane >> 4)) * 2) = f32x2{m, l};
  __syncthreads();
#pragma unroll
  for (int g = 0; g < 2; ++g) {
    f32x2 ml[RECS], a[RECS];
#pragma unroll
    for (int r = 0; r < RECS; ++r) ml[r] = *reinterpret_cast<const f32x2*>(lml + (r * 2 + g) * 2);
#pragma unroll
    for (int r = 0; r < RECS; ++r) a[r] = *reinterpret_cast<const f32x2*>(lds + (r * 2 + g) * D + 2 * tid);
    float M = ml[0].x;
#pragma unroll
    for (int r = 1; r < RECS; ++r) M = fmaxf(M, ml[r].x);
    const float Ms = safe_max(M);
    float L = 0.f; f32x2 o = {0.f, 0.f};
#pragma unroll
    for (int r = 0; r < RECS; ++r) { const float wt = __builtin_amdgcn_exp2f(ml[r].x - Ms); L = fmaf(wt, ml[r].y, L); o += f32x2{wt, wt} * a[r]; }
    const unsigned x = __float_as_uint((lane & 1) ? L : M);
    __builtin_amdgcn_raw_buffer_store_b128(u32x4{__float_as_uint(o.x), (x << 16) | (tag & 0xffffu), __float_as_uint(o.y), (x & 0xffff0000u) | (tag >> 16)},
                                           wsr, slot, g * (NSPL / 2) * NSPL * 128, AUX_SC1);
  }
  if (wave == WAVES - 1) {
    u32x4 gr[NK];
    unsigned off[NK], pend = (1u << NK) - 1u;
#pragma unroll
    for (int k = 0; k < NK; ++k) { gr[k] = u32x4{0u, 0u, 0u, 0u}; off[k] = doff + k * 1024u; }
#pragma nounroll
    for (unsigned spin = 0; spin < SPIN_CAP; ++spin) {
      u32x4 dv[NK];
#pragma unroll
      for (int k = 0; k < NK; ++k) dv[k] = __builtin_amdgcn_raw_buffer_load_b128(wsr, off[k], 0, AUX_SC1);
      asm volatile("" ::: "memory");
      __builtin_amdgcn_sched_barrier(0);
      unsigned bad = 0;
#pragma unroll
      for (int k = 0; k < NK; ++k) bad |= min(__builtin_amdgcn_perm(dv[k].w, dv[k].y, 0x05040100u) ^ tag, 1u) << k;
      bad |= dppu<XOR1>(bad); bad |= dppu<XOR2>(bad); bad |= dppu<XOR7>(bad);
      const unsigned got = pend & ~bad;
      if (__any(got != 0u)) {
        pend &= bad;
#pragma unroll
        for (int k = 0; k < NK; ++k) {
          const bool take = (got >> k) & 1u;
#pragma unroll
          for (int j = 0; j < 4; ++j) gr[k][j] = take ? dv[k][j] : gr[k][j];
          off[k] = take ? WS_BYTES : off[k];
        }
        if (!__any(pend != 0u)) break;
      }
    }
    float x[NK], pm[NK];
#pragma unroll
    for (int k = 0; k < NK; ++k) { x[k] = __uint_as_float((gr[k].y >> 16) | (gr[k].w & 0xffff0000u)); pm[k] = dpp<EVEN>(x[k]); }
    float M = pm[0];
#pragma unroll
    for (int k = 1; k < NK; ++k) M = fmaxf(M, pm[k]);
    M = max_8apart(M);
    float L = 0.f, o0 = 0.f, o1 = 0.f;
#pragma unroll
    for (int k = 0; k < NK; ++k) {
      const float wt = __builtin_amdgcn_exp2f(pm[k] - M);
      L = fmaf(wt, dpp<ODD>(x[k]), L); o0 = fmaf(wt, __uint_as_float(gr[k].x), o0); o1 = fmaf(wt, __uint_as_float(gr[k].z), o1);
    }
    L = sum_8apart(L); o0 = sum_8apart(o0); o1 = sum_8apart(o1);
    const float inv = __builtin_amdgcn_rcpf(L);
    const bf16x2 r = {(__bf16)(o0 * inv), (__bf16)(o1 * inv)};
    if (lane < 8) __hip_atomic_store(out + blockIdx.x * 8 + lane, __builtin_bit_cast(unsigned, r), __ATOMIC_RELAXED, __HIP_MEMORY_SCOPE_AGENT);
    if (s == 0 && lane == 0) __builtin_amdgcn_raw_buffer_store_b32(epoch + 1u, wsr, 0, head * 256, AUX_SC1);
  }
}

}

extern "C" void kernel_launch(void* const* d_in, const int* in_sizes, int n_in, void* d_out, int out_size,
                              void* d_ws, size_t ws_size, hipStream_t stream) {
  if (n_in != 5 || in_sizes[0] != 2 * HKV * D || in_sizes[1] != NUM_PHYS * PAGE * HKV * D || in_sizes[2] != in_sizes[1] ||
      in_sizes[3] != NBPS || in_sizes[4] != 1 || out_size != 2 * HKV * D || ws_size < WS_BYTES) return;
  attn_decode<<<NWG, NTHR, 0, stream>>>((const int*)d_in[3], (const uint16_t*)d_in[1], (const uint16_t*)d_in[2], (const int*)d_in[4],
                                        (unsigned char*)d_ws, (const uint16_t*)d_in[0], (unsigned*)d_out);
}
```
